```python
import torch

def _softmax(x, axis=-1):
    return torch.softmax(x, dim=axis)
def _gelu(x):
    return torch.nn.functional.gelu(x, approximate="tanh")
def _silu(x):
    return torch.nn.functional.silu(x)
def _sigmoid(x):
    return torch.sigmoid(x)
def _layernorm(x, axis=-1, eps=1e-5):
    m = x.mean(dim=axis, keepdim=True)
    v = x.var(dim=axis, keepdim=True, unbiased=False)
    return (x - m) / torch.sqrt(v + eps)
def _scatter_add(tgt, idx, src, axis=0):
    return tgt.clone().index_add_(axis, idx, src)
def _segment_sum(src, seg, n):
    out = torch.zeros((n,) + src.shape[1:], dtype=src.dtype)
    return out.index_add_(0, seg, src)
def _pad(x, pw):
    flat = tuple(v for pair in reversed(pw) for v in pair)
    return torch.nn.functional.pad(x, flat)

kernel_name = 'sequential_preLN__sliding_block_cached__mixer_channel_mlp__decode'

def setup_inputs():

    _w = (lambda a: (lambda t: t.to(torch.bfloat16) if t.is_floating_point() else t)(torch.as_tensor(a)))
    def _A_setup():
        B, Sq, D = 1, 1, 576
        Hq, Hkv, Sk, Dh = 32, 16, 65536, 128
        assert Hq % Hkv == 0, (Hq, Hkv)
        return {
            "x": _w((lambda *s: torch.randn(*s, dtype=torch.bfloat16) * 0.05)(B, Sq, D)),
            "k_cache": _w((lambda *s: torch.randn(*s, dtype=torch.bfloat16) * 3.2)(B, Hkv, Sk, Dh)),
            "v_cache": _w((lambda *s: torch.randn(*s, dtype=torch.bfloat16) * 0.4)(B, Hkv, Sk, Dh)),
            "wq": _w((lambda *s: torch.randn(*s, dtype=torch.bfloat16) * 3.2)(D, Hq * Dh) * (D ** -0.5)),
            "wo": _w((lambda *s: torch.randn(*s, dtype=torch.bfloat16) * 0.4)(Hq * Dh, D) * ((Hq * Dh) ** -0.5)),
        }
    _A_in = _A_setup()

    def _B_setup():
        B, S, D, F = 1, 1, 576, 4608
        return {
            "x": _w((lambda *s: torch.randn(*s, dtype=torch.bfloat16) * 0.05)(B, S, D)),
            "w1": _w((lambda *s: torch.randn(*s, dtype=torch.bfloat16) * 3.2)(D, F) * (D ** -0.5)),
            "b1": _w((lambda *s: torch.randn(*s, dtype=torch.bfloat16) * 3.2)(F)),
            "w2": _w((lambda *s: torch.randn(*s, dtype=torch.bfloat16) * 0.1)(F, D) * (F ** -0.5)),
            "b2": _w((lambda *s: torch.randn(*s, dtype=torch.bfloat16) * 0.1)(D)),
        }
    _B_in = _B_setup()

    return {
        "A_x": _A_in["x"],
        "A_k_cache": _A_in["k_cache"],
        "A_v_cache": _A_in["v_cache"],
        "A_wq": _A_in["wq"],
        "A_wo": _A_in["wo"],
        "B_x": _B_in["x"],
        "B_w1": _B_in["w1"],
        "B_b1": _B_in["b1"],
        "B_w2": _B_in["w2"],
        "B_b2": _B_in["b2"],
    }

def reference(A_x, A_k_cache, A_v_cache, A_wq, A_wo, B_x, B_w1, B_b1, B_w2, B_b2):
    def _A_ref(x, k_cache, v_cache, wq, wo):
        B, Sq, D = x.shape
        Hq, Dh = 32, 128
        Hkv, Sk = k_cache.shape[1], k_cache.shape[2]
        W = min(4096, max(256, Sk // 4))
        rep = Hq // Hkv
        q = (lambda x, *d: x.permute(*d))((x @ wq).reshape(B, Sq, Hq, Dh), 0, 2, 1, 3)
        k = (lambda x, n, axis: torch.repeat_interleave(x, n, dim=axis))(k_cache[:, :, Sk - W:, :], rep, axis=1)
        v = (lambda x, n, axis: torch.repeat_interleave(x, n, dim=axis))(v_cache[:, :, Sk - W:, :], rep, axis=1)
        scores = torch.einsum("bhqd,bhkd->bhqk", q, k) * (Dh ** -0.5)
        p = _softmax(scores, axis=-1)
        o = torch.einsum("bhqk,bhkd->bhqd", p, v)
        return (lambda x, *d: x.permute(*d))(o, 0, 2, 1, 3).reshape(B, Sq, Hq * Dh) @ wo

    def _B_ref(x, w1, b1, w2, b2):
        y = _layernorm(x, axis=-1)
        h = _gelu(torch.einsum("bsd,df->bsf", y, w1) + b1)
        return torch.einsum("bsf,fd->bsd", h, w2) + b2

    x = A_x
    h = x + _A_ref(_layernorm(x, axis=-1), A_k_cache, A_v_cache, A_wq, A_wo)
    h2 = h + _B_ref(_layernorm(h, axis=-1), B_w1, B_b1, B_w2, B_b2)
    return h2
```

```cpp
#include <hip/hip_runtime.h>
#include <hip/hip_bf16.h>
#include <stdint.h>

#define MIMIC 0

typedef unsigned short u16;
typedef unsigned int u32;
typedef u32 u32x4 __attribute__((ext_vector_type(4)));

constexpr int D = 576, HQ = 32, HKV = 16, SK = 65536, DH = 128, W = 4096, F = 4608;

__device__ __forceinline__ float bf2f(u16 v) { return __uint_as_float(((u32)v) << 16); }
__device__ __forceinline__ u16 f2bf(float f) { u32 u = __float_as_uint(f); u += 0x7fffu + ((u >> 16) & 1u); return (u16)(u >> 16); }
__device__ __forceinline__ float rbf(float f) {
  return f;
}

__device__ float block_sum(float v, float* red) {
  for (int o = 32; o >= 1; o >>= 1) v += __shfl_xor(v, o);
  int w = threadIdx.x >> 6, nw = blockDim.x >> 6;
  __syncthreads();
  if ((threadIdx.x & 63) == 0) red[w] = v;
  __syncthreads();
  float s = 0.f;
  for (int i = 0; i < nw; ++i) s += red[i];
  return s;
}

__device__ void ln_inplace(float* xs, float* red) {
  float s = 0.f;
  for (int i = threadIdx.x; i < D; i += blockDim.x) s += xs[i];
  s = block_sum(s, red);
  float mf = s / D;
  float q = 0.f;
  for (int i = threadIdx.x; i < D; i += blockDim.x) { float d = xs[i] - mf; q += d * d; }
  q = block_sum(q, red);
  float m = rbf(mf), v = rbf(q / D);
  float den = rbf(sqrtf(rbf(v + 1e-5f)));
  __syncthreads();
  for (int i = threadIdx.x; i < D; i += blockDim.x) xs[i] = rbf(rbf(xs[i] - m) / den);
  __syncthreads();
}

__global__ void __launch_bounds__(256) k_q(const u16* x, const u16* wq, float* q) {
  __shared__ float xs[D]; __shared__ float red[16]; __shared__ float part[4][64];
  for (int i = threadIdx.x; i < D; i += 256) xs[i] = bf2f(x[i]);
  __syncthreads();
  ln_inplace(xs, red);
  int col = blockIdx.x * 64 + (threadIdx.x & 63), w = threadIdx.x >> 6;
  float acc = 0.f;
  for (int k = w; k < D; k += 4) acc += xs[k] * bf2f(wq[(size_t)k * 4096 + col]);
  part[w][threadIdx.x & 63] = acc;
  __syncthreads();
  if (w == 0) q[col] = rbf(part[0][threadIdx.x] + part[1][threadIdx.x] + part[2][threadIdx.x] + part[3][threadIdx.x]);
}

__global__ void __launch_bounds__(256) k_attn(const float* q, const u16* kc, const u16* vc, float* pm, float* pl, float* po) {
  __shared__ float qs[256]; __shared__ float p[2][256]; __shared__ float red[16];
  int s = blockIdx.x, g = blockIdx.y, t = threadIdx.x;
  qs[t] = q[g * 256 + t];
  __syncthreads();
  const u16* kr = kc + ((size_t)g * SK + (SK - W) + s * 256 + t) * DH;
  float a0 = 0.f, a1 = 0.f;
  for (int d = 0; d < DH; d += 8) {
    u32x4 v = *(const u32x4*)(kr + d);
    for (int j = 0; j < 4; ++j) {
      float lo = __uint_as_float(v[j] << 16), hi = __uint_as_float(v[j] & 0xffff0000u);
      a0 += lo * qs[d + 2 * j] + hi * qs[d + 2 * j + 1];
      a1 += lo * qs[128 + d + 2 * j] + hi * qs[128 + d + 2 * j + 1];
    }
  }
  const float sc = 0.08838834764831845f;
  a0 = rbf(rbf(a0) * sc); a1 = rbf(rbf(a1) * sc);
  float m0 = a0, m1 = a1;
  for (int o = 32; o >= 1; o >>= 1) { m0 = fmaxf(m0, __shfl_xor(m0, o)); m1 = fmaxf(m1, __shfl_xor(m1, o)); }
  if ((t & 63) == 0) { red[t >> 6] = m0; red[4 + (t >> 6)] = m1; }
  __syncthreads();
  m0 = fmaxf(fmaxf(red[0], red[1]), fmaxf(red[2], red[3]));
  m1 = fmaxf(fmaxf(red[4], red[5]), fmaxf(red[6], red[7]));
  float e0 = __expf(a0 - m0), e1 = __expf(a1 - m1);
  p[0][t] = e0; p[1][t] = e1;
  float l0 = block_sum(e0, red);
  float l1 = block_sum(e1, red);
  __syncthreads();
  int h = t >> 7, d = t & 127;
  const u16* vr = vc + ((size_t)g * SK + (SK - W) + s * 256) * DH + d;
  float acc = 0.f;
  for (int k = 0; k < 256; ++k) acc += p[h][k] * bf2f(vr[(size_t)k * DH]);
  po[((size_t)(g * 2 + h) * 16 + s) * 128 + d] = acc;
  if (t == 0) { pm[(g * 2) * 16 + s] = m0; pm[(g * 2 + 1) * 16 + s] = m1; pl[(g * 2) * 16 + s] = l0; pl[(g * 2 + 1) * 16 + s] = l1; }
}

__global__ void __launch_bounds__(576) k_wo(const float* pm, const float* pl, const float* po, const u16* wo, float* part) {
  __shared__ float os[128];
  int h = blockIdx.x, t = threadIdx.x;
  if (t < 128) {
    float M = -INFINITY;
    for (int s = 0; s < 16; ++s) M = fmaxf(M, pm[h * 16 + s]);
    float L = 0.f, o = 0.f;
    for (int s = 0; s < 16; ++s) { float w = __expf(pm[h * 16 + s] - M); L += w * pl[h * 16 + s]; o += w * po[((size_t)h * 16 + s) * 128 + t]; }
    os[t] = rbf(o / L);
  }
  __syncthreads();
  float acc = 0.f;
  for (int d = 0; d < 128; ++d) acc += os[d] * bf2f(wo[(size_t)(h * 128 + d) * D + t]);
  part[h * D + t] = acc;
}

__global__ void __launch_bounds__(256) k_w1(const u16* x, const float* part, const u16* w1, const u16* b1, float* hbuf, float* g) {
  __shared__ float xs[D]; __shared__ float red[16]; __shared__ float pp[4][64];
  for (int i = threadIdx.x; i < D; i += 256) {
    float a = 0.f;
    for (int h = 0; h < 32; ++h) a += part[h * D + i];
    float hv = rbf(bf2f(x[i]) + rbf(a));
    xs[i] = hv;
    if (blockIdx.x == 0) hbuf[i] = hv;
  }
  __syncthreads();
  ln_inplace(xs, red);
  ln_inplace(xs, red);
  int col = blockIdx.x * 64 + (threadIdx.x & 63), w = threadIdx.x >> 6;
  float acc = 0.f;
  for (int k = w; k < D; k += 4) acc += xs[k] * bf2f(w1[(size_t)k * F + col]);
  pp[w][threadIdx.x & 63] = acc;
  __syncthreads();
  if (w == 0) {
    float u = rbf(rbf(pp[0][threadIdx.x] + pp[1][threadIdx.x] + pp[2][threadIdx.x] + pp[3][threadIdx.x]) + bf2f(b1[col]));
    float inner = 0.7978845608028654f * (u + 0.044715f * u * u * u);
    g[col] = rbf(0.5f * u * (1.f + tanhf(inner)));
  }
}

__global__ void __launch_bounds__(576) k_w2(const float* g, const u16* w2, float* part2) {
  __shared__ float gs[128];
  int b = blockIdx.x, t = threadIdx.x;
  if (t < 128) gs[t] = g[b * 128 + t];
  __syncthreads();
  float acc = 0.f;
  for (int d = 0; d < 128; ++d) acc += gs[d] * bf2f(w2[(size_t)(b * 128 + d) * D + t]);
  part2[b * D + t] = acc;
}

__global__ void __launch_bounds__(576) k_out(const float* part2, const float* hbuf, const u16* b2, u16* out) {
  int t = threadIdx.x;
  float a = 0.f;
  for (int b = 0; b < 36; ++b) a += part2[b * D + t];
  out[t] = f2bf(hbuf[t] + rbf(rbf(a) + bf2f(b2[t])));
}

extern "C" void kernel_launch(void* const* d_in, const int* in_sizes, int n_in,
                              void* d_out, int out_size, void* d_ws, size_t ws_size,
                              hipStream_t stream) {
  const u16* x = (const u16*)d_in[0];
  const u16* kc = (const u16*)d_in[1];
  const u16* vc = (const u16*)d_in[2];
  const u16* wq = (const u16*)d_in[3];
  const u16* wo = (const u16*)d_in[4];
  const u16* w1 = (const u16*)d_in[6];
  const u16* b1 = (const u16*)d_in[7];
  const u16* w2 = (const u16*)d_in[8];
  const u16* b2 = (const u16*)d_in[9];
  float* ws = (float*)d_ws;
  float* q = ws;
  float* pm = ws + 4096;
  float* pl = pm + 512;
  float* po = pl + 512;
  float* part = po + 65536;
  float* hbuf = part + 32 * 576;
  float* g = hbuf + 1024;
  float* part2 = g + 4608;
  k_q<<<64, 256, 0, stream>>>(x, wq, q);
  k_attn<<<dim3(16, 16), 256, 0, stream>>>(q, kc, vc, pm, pl, po);
  k_wo<<<32, 576, 0, stream>>>(pm, pl, po, wo, part);
  k_w1<<<72, 256, 0, stream>>>(x, part, w1, b1, hbuf, g);
  k_w2<<<36, 576, 0, stream>>>(g, w2, part2);
  k_out<<<1, 576, 0, stream>>>(part2, hbuf, b2, (u16*)d_out);
}
```

```cpp
#include <hip/hip_runtime.h>
#include <stdint.h>

typedef unsigned short u16;
typedef unsigned int u32;
typedef unsigned long long u64;
typedef u32 u32x2 __attribute__((ext_vector_type(2)));
typedef u32 u32x4 __attribute__((ext_vector_type(4)));
typedef u32 u32x8 __attribute__((ext_vector_type(8)));
typedef u32 u32x16 __attribute__((ext_vector_type(16)));
typedef float f32x2 __attribute__((ext_vector_type(2)));
typedef float f32x4 __attribute__((ext_vector_type(4)));
typedef short s16x4 __attribute__((ext_vector_type(4)));
typedef __bf16 bf16x2 __attribute__((ext_vector_type(2)));
typedef __bf16 bf16x8 __attribute__((ext_vector_type(8)));
#define LAS __attribute__((address_space(3)))
#define CAS __attribute__((address_space(4)))
#define RSRC __amdgpu_buffer_rsrc_t

namespace {

constexpr int NTHR = 9 * 64;
constexpr int SK = 65536, WIN = 4096;
constexpr int CODE_BYTES = 16128;
constexpr int PACE_K = 6, PACE_K_LATE = 8, SIG_K = 4, PACE_V = 9, SIG_V = 5, WO_PRE = 3, WO_GO = 3, PACE_W1 = 8, W1_GO = 2;
constexpr int W1_HELP_ROW = 320;

constexpr int WS_EPOCH = 0;
constexpr int WS_PR = 16384;
constexpr int HSTR = 16384 + 128;
constexpr int WS_H = WS_PR + 65536;
constexpr int WS_G = WS_H + 17 * 16384;
constexpr int WS_P = WS_G + 3 * 16384;
constexpr int WS_Q = WS_P + 48 * 16384;
constexpr int WS_O = WS_Q + 16 * 4096;
constexpr int WS_ML = WS_O + 256 * 1024;
constexpr int WS_END = WS_ML + 256 * 128;
static_assert(WS_END <= (2 << 20), "one 2 MiB page");
#define P_OFF(p) (WS_P + ((p) / 3) * 16384 + ((p) % 3) * 2304 + (((p) / 3) & 7) * 128)
#define G_OFF(n) (WS_G + ((n) >> 6) * 16384 + ((n) & 63) * 128)

constexpr int F_Q = 0, L_TAG = 4, L_HALF = 8, F_KV = 16, F_E = 32, C_P = 36, F_VD = 48, C_W = 64, C_O = 68, F_KQ = 72, C_ML = 76, C_G = 80,
              F_PUB = 84, C_W2 = 88, F_HS = 96, L_LAT = 112;
constexpr int L_MX = 128;
constexpr int L_LS = 160;
constexpr int L_E = 320;
constexpr int L_Q = 512;
constexpr int L_P = 1024;
constexpr int L_VEC = 2048;
constexpr int L_ONES = L_VEC + 1152;
constexpr int L_SINK = 3584;
constexpr int L_PART = 4096;
constexpr int L_X = 9728;
constexpr int L_K = 16384;
constexpr int L_W = 81920;
constexpr int LDS_BYTES = 163840;

constexpr int AUX_NT = 2, AUX_SC1 = 16;
constexpr int SPIN_CAP = 1 << 17;
constexpr int OOB = 0x7ffffff0;
constexpr float LOG2E = 1.4426950408889634f;
constexpr u32 ONES = 0x3f803f80u;

__device__ __forceinline__ RSRC mkrsrc(const void* p, int bytes) { return __builtin_amdgcn_make_buffer_rsrc((void*)p, 0, bytes, 0x00020000); }
__device__ __forceinline__ u32x4 mkdesc(const void* p, int bytes) { const u64 a = (u64)p; return u32x4{(u32)a, (u32)(a >> 32) & 0xffffu, (u32)bytes, 0x00020000u}; }
__device__ __forceinline__ float asf(u32 u) { return __builtin_bit_cast(float, u); }
__device__ __forceinline__ u32 asu(float f) { return __builtin_bit_cast(u32, f); }
__device__ __forceinline__ u32 pk_bf16(float lo, float hi) {
  bf16x2 r = {(__bf16)lo, (__bf16)hi};
  return __builtin_bit_cast(u32, r);
}
__device__ __forceinline__ float rbf(float f) { return asf(pk_bf16(f, f) & 0xffff0000u); }
__device__ __forceinline__ float lo_f(u32 w) { return asf(w << 16); }
__device__ __forceinline__ float hi_f(u32 w) { return asf(w & 0xffff0000u); }
__device__ __forceinline__ u32 tagw(float f, u32 T) { return __builtin_amdgcn_perm(asu(f), T, 0x07060500u); }
__device__ __forceinline__ u32x4 pack4(f32x4 v, u32 T) { return u32x4{asu(v.x), tagw(v.y, T), asu(v.z), tagw(v.w, T)}; }
__device__ __forceinline__ f32x4 untag(u32x4 w, u32 T, u32& bad) {
  const u32 y = w.y ^ T, z = w.w ^ T;
  bad |= y | z;
  return f32x4{asf(w.x), asf(y), asf(w.z), asf(z)};
}
__device__ __forceinline__ f32x4 mfma(u32x4 a, u32x4 b, f32x4 c) {
  return __builtin_amdgcn_mfma_f32_16x16x32_bf16(__builtin_bit_cast(bf16x8, a), __builtin_bit_cast(bf16x8, b), c, 0, 0, 0);
}
__device__ __forceinline__ float dot2(u32 a, u32 b, float c) {
  return __builtin_amdgcn_fdot2_f32_bf16(__builtin_bit_cast(bf16x2, a), __builtin_bit_cast(bf16x2, b), c, false);
}
template <int CTRL> __device__ __forceinline__ float dpp(float x) { return asf((u32)__builtin_amdgcn_mov_dpp((int)asu(x), CTRL, 0xf, 0xf, true)); }
constexpr int XOR1 = 0xB1, XOR2 = 0x4E, ROR4 = 0x124, ROR8 = 0x128;
__device__ __forceinline__ float sum32(float a, float b) { auto s = __builtin_amdgcn_permlane32_swap(asu(a), asu(b), false, false); return asf(s[0]) + asf(s[1]); }
__device__ __forceinline__ float sum16(float a, float b) { auto s = __builtin_amdgcn_permlane16_swap(asu(a), asu(b), false, false); return asf(s[0]) + asf(s[1]); }
__device__ __forceinline__ float max32(float a) { auto s = __builtin_amdgcn_permlane32_swap(asu(a), asu(a), false, false); return fmaxf(asf(s[0]), asf(s[1])); }
__device__ __forceinline__ float max16(float a) { auto s = __builtin_amdgcn_permlane16_swap(asu(a), asu(a), false, false); return fmaxf(asf(s[0]), asf(s[1])); }
__device__ __forceinline__ float tred4(f32x4 v) { return sum16(sum32(v.x, v.z), sum32(v.y, v.w)); }
__device__ __forceinline__ float wave_sum(float v) {
  v += dpp<XOR1>(v); v += dpp<XOR2>(v); v += dpp<ROR4>(v); v += dpp<ROR8>(v);
  v = sum16(v, v); return sum32(v, v);
}
__device__ __forceinline__ int swap23(int k) { return (k & ~12) | ((k & 4) << 1) | ((k & 8) >> 1); }

#define BAR() asm volatile("s_waitcnt lgkmcnt(0)\n\ts_barrier" ::: "memory")
#define FENCE() asm volatile("" ::: "memory")
#define VMWAIT(n) asm volatile("s_waitcnt vmcnt(%0)" :: "n"(n) : "memory")
#define PIN(x) asm volatile("" : "+v"(x))

template <typename T> __device__ __forceinline__ T lds_ld(int off) { return *(const LAS T*)(size_t)(u32)off; }
template <typename T> __device__ __forceinline__ void lds_st(int off, T v) { *(LAS T*)(size_t)(u32)off = v; }
__device__ __forceinline__ u32x4 tr_frag(int off) {
  s16x4 a = __builtin_amdgcn_ds_read_tr16_b64_v4i16((LAS s16x4*)(size_t)(u32)off);
  s16x4 b = __builtin_amdgcn_ds_read_tr16_b64_v4i16((LAS s16x4*)(size_t)(u32)(off + 1024));
  u32x2 x = __builtin_bit_cast(u32x2, a), y = __builtin_bit_cast(u32x2, b);
  return u32x4{x.x, x.y, y.x, y.y};
}
__device__ __forceinline__ void dma1(u32x4 rs, int voff, int soff, int lds) {
  asm volatile("s_mov_b32 m0, %3\n\ts_nop 0\n\tbuffer_load_dwordx4 %0, %1, %2 offen nt lds" :: "v"(voff), "s"(rs), "s"(soff), "s"(lds) : "memory");
}
__device__ __forceinline__ void pf1(u32x4 rs, int voff) {
  asm volatile("s_mov_b32 m0, %2\n\ts_nop 0\n\tbuffer_load_dword %0, %1, 0 offen lds" :: "v"(voff), "s"(rs), "s"(L_SINK) : "memory");
}
template <int N, int P> __device__ __forceinline__ void dma_tiles(u32x4 rs, int voff, int soff, int sstep, int lds, int lstep) {
#pragma unroll
  for (int i = 0; i < N; ++i) { if (i >= P) VMWAIT(P - 1); dma1(rs, voff, soff + i * sstep, lds + i * lstep); }
}
__device__ __forceinline__ u32 flag_ld(int off) { return __builtin_amdgcn_readfirstlane(*(volatile LAS u32*)(size_t)(u32)off); }
__device__ __forceinline__ void flag_st(int off, u32 v) { FENCE(); *(volatile LAS u32*)(size_t)(u32)off = v; FENCE(); }
__device__ __forceinline__ void flag_inc(int off) {
  asm volatile("s_mov_b64 exec, 1\n\tds_add_u32 %0, %1\n\ts_mov_b64 exec, -1" :: "v"(off), "v"(1u) : "memory");
}
__device__ __forceinline__ void flag_wait(int off, u32 n) {
  int n_ = 1 << 22;
#pragma unroll 1
  do { if (flag_ld(off) >= n) break; __builtin_amdgcn_s_sleep(1); } while (--n_);
  FENCE();
}
__device__ __forceinline__ void flag_spin(int off, u32 n) {
  int c_; u32 t_;
  asm volatile("s_mov_b32 %0, 0x400000\n"
               "1:\n\tds_read_b32 %1, %2\n\ts_sub_u32 %0, %0, 1\n\ts_cbranch_scc1 2f\n\ts_waitcnt lgkmcnt(0)\n\tv_cmp_gt_u32 vcc, %3, %1\n\ts_cbranch_vccnz 1b\n"
               "2:\n\ts_waitcnt lgkmcnt(0)"
               : "=&s"(c_), "=&v"(t_) : "v"(off), "s"(n) : "vcc", "scc", "memory");
}
__device__ __forceinline__ int near_half() { return *(volatile LAS u32*)(size_t)(u32)L_HALF; }

template <int N, int SL> __device__ __forceinline__ f32x4 poll_sum(RSRC rs, int voff, int sstep, u32 T) {
  f32x4 r;
  int n_ = SPIN_CAP;
#pragma unroll 1
  do {
    FENCE();
    u32x4 d[N];
#pragma unroll
    for (int i = 0; i < N; ++i) d[i] = __builtin_amdgcn_raw_buffer_load_b128(rs, voff, i * sstep, AUX_SC1);
    u32 bad = 0;
    if (N == 4) r = (untag(d[0], T, bad) + untag(d[1 % N], T, bad)) + (untag(d[2 % N], T, bad) + untag(d[3 % N], T, bad));
    else r = untag(d[0], T, bad);
    if (__builtin_amdgcn_ballot_w64((bad & 0xffu) != 0) == 0) break;
    if (SL) __builtin_amdgcn_s_sleep(SL);
  } while (--n_);
  return r;
}
#define TRED4_ASM "s_nop 0\n\tv_permlane32_swap_b32 v140, v142\n\tv_permlane32_swap_b32 v141, v143\n\tv_add_f32 v140, v140, v142\n\tv_add_f32 v141, v141, v143\n\t" \
                  "s_nop 1\n\tv_permlane16_swap_b32 v140, v141\n\tv_add_f32 v140, v140, v141\n\t"
#define ROR_ADD(n) "s_nop 1\n\tv_add_f32_dpp v140, v140, v140 row_ror:" #n " row_mask:0xf bank_mask:0xf\n\t"
#define BAR_ASM "s_waitcnt lgkmcnt(0)\n\ts_barrier"
#define POLL_CLOB "vcc", "scc", "memory", "v140", "v141", "v142", "v143", "v144", "v145", "v146", "v147", "v156"
#define LOAD4_ASM(a, r, s1, s2, s3) "buffer_load_dwordx4 v[140:143], " a ", " r ", 0 offen sc1\n\tbuffer_load_dwordx4 v[144:147], " a ", " r ", " s1 " offen sc1\n\t" \
               "buffer_load_dwordx4 v[148:151], " a ", " r ", " s2 " offen sc1\n\tbuffer_load_dwordx4 v[152:155], " a ", " r ", " s3 " offen sc1\n"
__device__ __forceinline__ u32x16 poll4_issue(u32x4 rs, int voff, int s1, int s2, int s3) {
  u32x16 d;
  asm volatile(LOAD4_ASM("%1", "%2", "%3", "%4", "%5") : "={v[140:155]}"(d) : "v"(voff), "s"(rs), "s"(s1), "s"(s2), "s"(s3) : "memory");
  return d;
}
__device__ __forceinline__ void poll4_h(u32x4 rs, int voff, int s1, int s2, int s3, u32 T, float xv, int dst, u32x16 d) {
  int n, m;
  asm volatile("s_mov_b32 %1, %9\n\ts_branch 2f\n"
               "1:\n\t" LOAD4_ASM("%3", "%4", "%5", "%6", "%7")
               "2:\n\t"
               "s_sub_u32 %1, %1, 1\n\ts_cselect_b32 %2, 0, 0xff\n\t"
               "s_waitcnt vmcnt(2)\n\t"
               "v_xor_b32 v141, %8, v141\n\tv_xor_b32 v143, %8, v143\n\tv_xor_b32 v145, %8, v145\n\tv_xor_b32 v147, %8, v147\n\t"
               "v_or3_b32 v156, v141, v143, v145\n\tv_pk_add_f32 v[140:141], v[140:141], v[144:145]\n\tv_pk_add_f32 v[142:143], v[142:143], v[146:147]\n\t"
               "s_waitcnt vmcnt(1)\n\t"
               "v_xor_b32 v149, %8, v149\n\tv_xor_b32 v151, %8, v151\n\tv_or3_b32 v156, v156, v147, v149\n\t"
               "v_pk_add_f32 v[140:141], v[140:141], v[148:149]\n\tv_pk_add_f32 v[142:143], v[142:143], v[150:151]\n\t"
               "s_waitcnt vmcnt(0)\n\t"
               "v_xor_b32 v153, %8, v153\n\tv_xor_b32 v155, %8, v155\n\tv_or3_b32 v156, v156, v151, v153\n\t"
               "v_bitop3_b32 v156, v156, %2, v155 bitop3:0xc8\n\tv_cmp_ne_u32 vcc, 0, v156\n\t"
               "v_pk_add_f32 v[140:141], v[140:141], v[152:153]\n\tv_pk_add_f32 v[142:143], v[142:143], v[154:155]\n\t"
               "s_cbranch_vccnz 1b\n\t" TRED4_ASM
               "v_cvt_pk_bf16_f32 v140, 0, v140\n\tv_add_f32 v140, v140, %11\n\tv_cvt_pk_bf16_f32 v140, v140, v140\n\tds_write_b16 %10, v140\n\t" BAR_ASM
               : "+{v[140:155]}"(d), "=&s"(n), "=&s"(m) : "v"(voff), "s"(rs), "s"(s1), "s"(s2), "s"(s3), "s"(T), "n"(SPIN_CAP), "v"(dst), "v"(xv)
               : "vcc", "scc", "memory", "v156");
}
__device__ __forceinline__ void poll1_p(u32x4 rs, int voff, u32 T, int dst) {
  int n, m;
  asm volatile("s_mov_b32 %0, %6\n"
               "1:\n\t"
               "buffer_load_dwordx4 v[140:143], %3, %4, 0 offen sc1\n\t"
               "s_sub_u32 %0, %0, 1\n\ts_cselect_b32 %1, 0, 0xff\n\t"
               "s_waitcnt vmcnt(0)\n\t"
               "v_xor_b32 v141, %5, v141\n\tv_xor_b32 v143, %5, v143\n\t"
               "v_bitop3_b32 v156, v141, %1, v143 bitop3:0xc8\n\tv_cmp_ne_u32 vcc, 0, v156\n\t"
               "s_cbranch_vccnz 1b\n\t" TRED4_ASM ROR_ADD(8) ROR_ADD(4) "ds_write_b32 %2, v140\n\t" BAR_ASM
               : "=&s"(n), "=&s"(m) : "v"(dst), "v"(voff), "s"(rs), "s"(T), "n"(SPIN_CAP) : POLL_CLOB);
}
__device__ __forceinline__ u32x8 poll2_issue(u32x4 rs, int voff, int s1) {
  u32x8 d;
  asm volatile("buffer_load_dwordx4 v[144:147], %1, %2, 0 offen sc1\n\tbuffer_load_dwordx4 v[148:151], %1, %2, %3 offen sc1"
               : "={v[144:151]}"(d) : "v"(voff), "s"(rs), "s"(s1) : "memory");
  return d;
}
__device__ __forceinline__ void poll2_o(u32x4 rs, int voff, int s1, u32 T, f32x2 e0, f32x2 e1, int dst, u32x8 d) {
  int n, m;
  asm volatile("s_mov_b32 %1, %9\n\ts_branch 2f\n"
               "1:\n\t"
               "buffer_load_dwordx4 v[144:147], %3, %4, 0 offen sc1\n\tbuffer_load_dwordx4 v[148:151], %3, %4, %5 offen sc1\n"
               "2:\n\t"
               "s_sub_u32 %1, %1, 1\n\ts_cselect_b32 %2, 0, 0xff\n\t"
               "s_waitcnt vmcnt(1)\n\t"
               "v_xor_b32 v145, %6, v145\n\tv_xor_b32 v147, %6, v147\n\t"
               "v_pk_mul_f32 v[140:141], v[144:145], %7\n\tv_pk_mul_f32 v[142:143], v[146:147], %7\n\t"
               "s_waitcnt vmcnt(0)\n\t"
               "v_xor_b32 v149, %6, v149\n\tv_xor_b32 v151, %6, v151\n\tv_or3_b32 v156, v145, v147, v149\n\t"
               "v_bitop3_b32 v156, v156, %2, v151 bitop3:0xc8\n\tv_cmp_ne_u32 vcc, 0, v156\n\t"
               "v_pk_fma_f32 v[140:141], v[148:149], %8, v[140:141]\n\tv_pk_fma_f32 v[142:143], v[150:151], %8, v[142:143]\n\t"
               "s_cbranch_vccnz 1b\n\t" TRED4_ASM ROR_ADD(8) "v_cvt_pk_bf16_f32 v140, v140, v140\n\tds_write_b16 %10, v140\n\t" BAR_ASM
               : "+{v[144:151]}"(d), "=&s"(n), "=&s"(m) : "v"(voff), "s"(rs), "s"(s1), "s"(T), "v"(e0), "v"(e1), "n"(SPIN_CAP), "v"(dst)
               : "vcc", "scc", "memory", "v140", "v141", "v142", "v143", "v156");
}

template <typename E, typename F> __device__ __forceinline__ void gather_o(u32x4 dws, int g, int aw, int l, u32 T, E pre, F mid) {
  const int l3 = l >> 3, l4 = l >> 4;
  int oo = WS_O + (g * 16 + l3) * 1024 + (aw * 8 + (l & 7)) * 16;
  int odst = L_VEC + (aw * 32 + swap23(4 * (l & 7) + l4)) * 2;
  int ea = L_E + (aw >> 2) * 64 + l3 * 4;
  PIN(odst); PIN(oo); PIN(ea);
  pre();
  const u32x8 d = poll2_issue(dws, oo, 8192);
  flag_spin(F_E, 1u);
  const float e0 = lds_ld<float>(ea), e1 = lds_ld<float>(ea + 32);
  f32x2 e00 = {e0, e0}, e11 = {e1, e1};
  PIN(e00); PIN(e11);
  mid();
  poll2_o(dws, oo, 8192, T, e00, e11, odst, d);
}

template <typename E, typename F> __device__ __forceinline__ void gather_h(u32x4 dws, int cb, int l, u32 T, E pre, F mid) {
  const int l4 = l >> 4;
  flag_wait(F_E, 1u);
  float xv = lo_f(lds_ld<u16>(L_X + (64 * cb + 4 * (l & 15) + l4) * 2));
  int ho = WS_H + near_half() + l4 * HSTR + cb * 256 + (l & 15) * 16;
  int hdst = L_VEC + cb * 128 + swap23(4 * (l & 15) + l4) * 2;
  PIN(ho); PIN(hdst); PIN(xv);
  pre();
  const u32x16 d = poll4_issue(dws, ho, 4 * HSTR, 8 * HSTR, 12 * HSTR);
  mid();
  poll4_h(dws, ho, 4 * HSTR, 8 * HSTR, 12 * HSTR, T, xv, hdst, d);
}

__global__ void __launch_bounds__(NTHR, 3) __attribute__((aligned(4096)))
fused_decode(const char*, const char*, const char*, const char*, char*, const char*, const char*, const char*, const char*, const char*, char*) {
  extern __shared__ __attribute__((aligned(16))) char smem_[];
  const u64 CAS* ka = (const u64 CAS*)__builtin_amdgcn_kernarg_segment_ptr();
  u32x8 a8; u64 ws_;
  asm volatile("s_load_dwordx8 %0, %2, 0\n\ts_load_dwordx2 %1, %2, 32" : "=&s"(a8), "=&s"(ws_) : "s"(ka));
  __builtin_amdgcn_sched_barrier(0);
  const int b = blockIdx.x, tid = threadIdx.x, l = tid & 63, wave = __builtin_amdgcn_readfirstlane(tid >> 6);
  if (wave == 8) {
    lds_st<u32>(l * 4, 0u);
    u64 pc; asm volatile("s_getpc_b64 %0" : "=s"(pc));
    pf1(mkdesc((const void*)(pc & ~4095ull), CODE_BYTES), l < 4 ? (l * 32 + (b >> 3)) * 128 : OOB);
  }
  __builtin_amdgcn_sched_barrier(0);
  const int xcd = b & 7, idx = b >> 3, g = xcd * 2 + (idx >> 4), s = idx & 15, cu = g * 16 + s;
  const int l3 = l >> 3, l4 = l >> 4;
  const int w4 = (wave - 1) & 3;
  const int gq = g ^ 2;
  const int cg = s & 3, rg = s >> 2;
  u64 kvb = ((u64)g * SK + (SK - WIN) + s * 256 + w4 * 16) * 256;
  const int kl = L_K + w4 * 16384;
  int qo = (rg * 144 + l3 * 18) * 8192 + (gq * 256 + cg * 64) * 2 + (l & 7) * 16, l16 = l * 16;
  int ks0 = l4 * 256 + (((l & 15) ^ l4) * 16), ks1 = ks0 ^ 64, ks2 = ks0 ^ 128, ks3 = ks0 ^ 192;
  PIN(qo); PIN(l16); PIN(ks0); PIN(ks1); PIN(ks2); PIN(ks3);
  asm volatile("" : "+s"(kvb));
  asm volatile("s_waitcnt lgkmcnt(0)" : "+s"(a8), "+s"(ws_) :: "memory");
  const char* x = (const char*)((u64)a8[0] | ((u64)a8[1] << 32));
  const char* kc = (const char*)((u64)a8[2] | ((u64)a8[3] << 32));
  const char* vc = (const char*)((u64)a8[4] | ((u64)a8[5] << 32));
  const char* wq = (const char*)((u64)a8[6] | ((u64)a8[7] << 32));
  char* ws = (char*)((ws_ + 0x1fffffull) & ~0x1fffffull);
#define ARG_wo ((const char*)ka[5])
#define ARG_w1 ((const char*)ka[6])
#define ARG_w2 ((const char*)ka[7])
#define ARG_b1 ((const char*)ka[8])
#define ARG_b2 ((const char*)ka[9])
#define ARG_out ((char*)ka[10])
  if (wave != 0) {
    u32x4 rk = mkdesc(kc + kvb, 53248);
    asm volatile("" : "+s"(rk));
    if (wave == 5 || wave == 6) {
      const int hf = (wave ^ s) & 1;
      u64 ta_, tb_; u32 x_;
      asm volatile("s_memtime %0\n\ts_load_dword %1, %3, 0 glc\n\ts_barrier\n\ts_waitcnt lgkmcnt(0)\n\ts_memtime %2\n\ts_waitcnt lgkmcnt(0)"
                   : "=&s"(ta_), "=&s"(x_), "=&s"(tb_) : "s"(ws + WS_PR + (b >> 6) * 16384 + hf * 8192 + (b & 63) * 128) : "memory");
      lds_st<u32>(L_LAT + hf * 4, (u32)tb_ - (u32)ta_);
    } else {
      asm volatile("s_barrier" ::: "memory");
    }
    if (wave <= 4) {
      const bool norm = (((a8[6] >> 7) + gq * 4 + cg) & 7) != 3;
#define KPACE() do { if (norm) VMWAIT(PACE_K - 1); VMWAIT(PACE_K_LATE - 1); } while (0)
#pragma unroll 1
      for (int n4 = 0; n4 < 16384; n4 += 4096) {
        const int so = (n4 * 4 + s * 16384) & 65535;
        KPACE(); dma1(rk, ks0, so, kl + n4);
        KPACE(); dma1(rk, ks1, so + 1024, kl + n4 + 1024);
        KPACE(); dma1(rk, ks2, so + 2048, kl + n4 + 2048);
        KPACE(); dma1(rk, ks3, so + 3072, kl + n4 + 3072);
      }
      flag_st(F_KQ, 1u);
    }
  }
  int wave2 = wave; asm volatile("" : "+s"(wave2));
  const RSRC rws = mkrsrc(ws, WS_END);
#define ROLEDEF const int rl = (((idx & 3) + (idx >> 4) * 4 + (int)(a8[6] >> 7) + 4) & 7) * 4 + ((idx >> 2) & 3); \
  const bool isM = rl < 9, isO = !isM && rl < 27; const int oh_ = rl >= 18, cb = rl - 9 - 9 * oh_, gn = (xcd ^ 1) * 2 + oh_; \
  const int mi = xcd * 9 + rl, ri = xcd * 5 + rl - 27, pi = (xcd ^ 1) * 18 + rl - 9; const u32x4 dws = mkdesc(ws, WS_END);
#define SELDEF u32 selLo[4], selHi[4]; \
  _Pragma("unroll") for (int t = 0; t < 4; ++t) { const bool on = (((l & 7) >> 1) == t); \
    selLo[t] = on ? ((l & 1) ? 0x01000c0cu : 0x0c0c0100u) : 0x0c0c0c0cu; selHi[t] = on ? ((l & 1) ? 0x03020c0cu : 0x0c0c0302u) : 0x0c0c0c0cu; \
    PIN(selLo[t]); PIN(selHi[t]); }
#define DIAG_LO(p) u32x4{__builtin_amdgcn_perm(0u, (p), selLo[0]), __builtin_amdgcn_perm(0u, (p), selLo[1]), __builtin_amdgcn_perm(0u, (p), selLo[2]), __builtin_amdgcn_perm(0u, (p), selLo[3])}
#define DIAG_HI(p) u32x4{__builtin_amdgcn_perm(0u, (p), selHi[0]), __builtin_amdgcn_perm(0u, (p), selHi[1]), __builtin_amdgcn_perm(0u, (p), selHi[2]), __builtin_amdgcn_perm(0u, (p), selHi[3])}
#define TRDEF const int r7 = 4 * (l4 & 1) + ((l >> 2) & 3); int ta = L_W + (16 * (l >> 5) + r7) * 128 + (l & 3) * 8 + ((w4 ^ (r7 >> 1)) * 32);
  u32 T32;
  float hv = 0.f, b2v = 0.f;

  if (__builtin_expect(wave2 == 0, 1)) {
    const RSRC rq = mkrsrc(wq, 576 * 8192), rx = mkrsrc(x, 1152);
    const u32x4 xa = __builtin_amdgcn_raw_buffer_load_b128(rx, l16, 0, 0);
    const u32x4 xb = __builtin_amdgcn_raw_buffer_load_b128(rx, l16 + 1024, 0, 0);
    __builtin_amdgcn_sched_barrier(0);
    u32x4 rr[18];
#pragma unroll
    for (int i = 0; i < 18; ++i) { rr[i] = __builtin_amdgcn_raw_buffer_load_b128(rq, qo, i * 8192, AUX_NT); __builtin_amdgcn_sched_barrier(0); }
    const u32 ep = __builtin_amdgcn_raw_buffer_load_b32(rws, WS_EPOCH, 0, AUX_SC1);
    asm volatile("s_barrier" ::: "memory");
    __builtin_amdgcn_sched_barrier(0);
    ROLEDEF
    SELDEF
    lds_st<u32x4>(L_X + l16, xa);
    if (l < 8) lds_st<u32x4>(L_X + 1024 + l16, xb);
    u32 xp[9];
#pragma unroll
    for (int j = 0; j < 9; ++j) xp[j] = lds_ld<u32>(L_X + rg * 288 + l3 * 36 + j * 4);
    int qdst = (l < 32 && !(l & 8)) ? WS_Q + gq * 4096 + rg * 1024 + cg * 256 + (l & 7) * 32 + (l4 & 1) * 16 : OOB;
    PIN(qdst);
    float sm = 0.f, sq = 0.f;
#pragma unroll
    for (int j = 0; j < 4; ++j) { sm = dot2(xa[j], ONES, sm); sq = dot2(xa[j], xa[j], sq); sm = dot2(xb[j], ONES, sm); sq = dot2(xb[j], xb[j], sq); }
    sm = wave_sum(sm); sq = wave_sum(sq);
    const float mean = sm * (1.f / 576.f);
    const float var = fmaxf(sq * (1.f / 576.f) - mean * mean, 0.f);
    const float mb = rbf(mean);
    const float rden = __builtin_amdgcn_rcpf(rbf(__builtin_amdgcn_sqrtf(rbf(rbf(var) + 1e-5f))));
    f32x4 acc = {0.f, 0.f, 0.f, 0.f};
#pragma unroll
    for (int j = 0; j < 9; ++j) {
      const u32 d = pk_bf16(lo_f(xp[j]) - mb, hi_f(xp[j]) - mb);
      const u32 n = pk_bf16(lo_f(d) * rden, hi_f(d) * rden);
      acc = mfma(DIAG_LO(n), rr[2 * j], acc);
      acc = mfma(DIAG_HI(n), rr[2 * j + 1], acc);
    }
    __builtin_amdgcn_sched_barrier(0);
    T32 = __builtin_amdgcn_readfirstlane(ep) + 1u;
    const u32 T = T32 & 0xffu;
    f32x4 qp;
#pragma unroll
    for (int r = 0; r < 4; ++r) {
      auto sw = __builtin_amdgcn_permlane32_swap(asu(acc[r]), asu(acc[r]), false, false);
      qp[r] = acc[r] + dpp<ROR8>(asf(sw[1]));
    }
    __builtin_amdgcn_raw_buffer_store_b128(pack4(qp, T), rws, qdst, 0, AUX_SC1);
    lds_st<u32>(L_TAG, T32);
    flag_wait(F_KQ, 1u);
    {
      const f32x4 q4 = poll_sum<4, 0>(rws, WS_Q + g * 4096 + l * 16, 1024, T);
      lds_st<u32x2>(L_Q + l * 8, u32x2{pk_bf16(q4.x, q4.y), pk_bf16(q4.z, q4.w)});
      flag_st(F_Q, 1u);
    }
    {
      flag_wait(C_P, 4u);
      const f32x4 m4 = lds_ld<f32x4>(L_MX + ((l >> 2) & 1) * 16), l4v = lds_ld<f32x4>(L_LS + ((l >> 2) & 1) * 16);
      const float mx = fmaxf(fmaxf(m4.x, m4.y), fmaxf(m4.z, m4.w));
      const float ls = (l4v.x * __builtin_amdgcn_exp2f((m4.x - mx) * LOG2E) + l4v.y * __builtin_amdgcn_exp2f((m4.y - mx) * LOG2E)) +
                       (l4v.z * __builtin_amdgcn_exp2f((m4.z - mx) * LOG2E) + l4v.w * __builtin_amdgcn_exp2f((m4.w - mx) * LOG2E));
      const u32x2 lt = *(volatile LAS u32x2*)(size_t)(u32)L_LAT;
      int vote = (int)lt.x - (int)lt.y;
      vote = vote > 1000 ? 1000 : vote < -1000 ? -1000 : vote;
      __builtin_amdgcn_raw_buffer_store_b128(u32x4{asu(mx) | ((u32)vote & 0xffffu), T32, asu(ls), T32}, rws, l < 8 ? WS_ML + cu * 128 + l * 16 : OOB, 0, AUX_SC1);
    }
    {
      flag_wait(C_ML, 4u);
      u32x4 m;
      int n_ = SPIN_CAP;
#pragma unroll 1
      do {
        FENCE();
        m = __builtin_amdgcn_raw_buffer_load_b128(rws, WS_ML + (gn * 16 + (l & 15)) * 128 + (l4 & 1) * 64, 0, AUX_SC1);
        if (!isO || __all(((m.y ^ T32) | (m.w ^ T32)) == 0)) break;
      } while (--n_);
      float vs = m.y == T32 ? (float)(int)(short)m.x : 0.f;
      m.x &= 0xffff0000u;
      vs += dpp<XOR1>(vs); vs += dpp<XOR2>(vs); vs += dpp<ROR4>(vs); vs += dpp<ROR8>(vs);
      lds_st<u32>(L_HALF, vs > 0.f ? 8192u : 0u);
      float M = asf(m.x);
      M = fmaxf(M, dpp<XOR1>(M)); M = fmaxf(M, dpp<XOR2>(M)); M = fmaxf(M, dpp<ROR4>(M)); M = fmaxf(M, dpp<ROR8>(M));
      const float e = __builtin_amdgcn_exp2f((asf(m.x) - M) * LOG2E);
      float L = e * asf(m.z);
      L += dpp<XOR1>(L); L += dpp<XOR2>(L); L += dpp<ROR4>(L); L += dpp<ROR8>(L);
      lds_st<float>(L_E + (l & 31) * 4, e * __builtin_amdgcn_rcpf(L));
      flag_st(F_E, 1u);
    }
    if (isO) {
      BAR();
    } else if (isM) {
      if (l < 24) lds_st<u32x4>(L_ONES + l * 16, u32x4{ONES, ONES, ONES, ONES});
      gather_h(dws, 0, l, T, []() { flag_spin(C_G, 4u); }, []() {});
      return;
    } else {
      if (ri >= 36) return;
      const int col = 16 * ri + 4 * (l & 3) + l4;
      b2v = lo_f(__builtin_amdgcn_raw_buffer_load_b16(mkrsrc(ARG_b2, 1152), col * 2, 0, 0));
      const float xv = lo_f(lds_ld<u16>(L_X + col * 2));
      PIN(b2v);
      flag_wait(C_W, 4u);
      float hz = tred4(poll_sum<1, 8>(rws, WS_H + near_half() + (l >> 2) * HSTR + ri * 64 + (l & 3) * 16, 0, T));
      hz += dpp<ROR8>(hz); hz += dpp<ROR4>(hz);
      hv = rbf(xv + rbf(hz));
    }
  } else if (wave2 <= 4) {
    VMWAIT(SIG_K);
    flag_st(F_KV + w4 * 4, 1u);
    ROLEDEF
    VMWAIT(0);
    u32x4 kf[16];
    {
      const int kr = kl + (l & 15) * 256 + ((l4 ^ (l & 15)) * 16);
#pragma unroll
      for (int n = 0; n < 16; ++n) kf[n] = lds_ld<u32x4>((kr ^ ((n & 3) * 64)) + (n >> 2) * 4096);
    }
    flag_wait(F_Q, 1u);
    T32 = flag_ld(L_TAG);
    const u32 T = T32 & 0xffu;
    const int hd = l3 & 1;
    f32x4 sc[4];
#pragma unroll
    for (int t = 0; t < 4; ++t) sc[t] = f32x4{0.f, 0.f, 0.f, 0.f};
#pragma unroll
    for (int c = 0; c < 4; ++c) {
      const u32x4 qf = lds_ld<u32x4>(L_Q + hd * 256 + c * 64 + l4 * 16);
#pragma unroll
      for (int t = 0; t < 4; ++t) sc[t] = mfma(kf[4 * t + c], qf, sc[t]);
    }
    constexpr float SCALE = 0.08838834764831845f;
    const bool bb0 = l & 1, bb1 = l & 2;
    float sv[4];
#pragma unroll
    for (int r = 0; r < 4; ++r) { const float lo = bb0 ? sc[1][r] : sc[0][r], hi = bb0 ? sc[3][r] : sc[2][r]; sv[r] = bb1 ? hi : lo; }
#pragma unroll
    for (int r = 0; r < 4; r += 2) {
      const u32 a = pk_bf16(sv[r], sv[r + 1]);
      const u32 c = pk_bf16(lo_f(a) * SCALE, hi_f(a) * SCALE);
      sv[r] = lo_f(c); sv[r + 1] = hi_f(c);
    }
    float mx = fmaxf(fmaxf(sv[0], sv[1]), fmaxf(sv[2], sv[3]));
    mx = fmaxf(mx, dpp<XOR1>(mx)); mx = fmaxf(mx, dpp<XOR2>(mx));
    mx = max32(max16(mx));
#pragma unroll
    for (int i = 0; i < 4; ++i) sv[i] = __builtin_amdgcn_exp2f((sv[i] - mx) * LOG2E);
    lds_st<u32x2>(L_P + w4 * 256 + l3 * 32 + (l & 3) * 8, u32x2{pk_bf16(sv[0], sv[1]), pk_bf16(sv[2], sv[3])});
    float ls = (sv[0] + sv[1]) + (sv[2] + sv[3]);
    ls += dpp<XOR1>(ls); ls += dpp<XOR2>(ls);
    ls = sum16(ls, ls); ls = sum32(ls, ls);
    lds_st<float>(L_MX + (l & 1) * (L_LS - L_MX) + hd * 16 + w4 * 4, (l & 1) ? ls : mx);
    flag_inc(C_P);
    const int sw16 = ((l & 7) ^ (l3 & 6)) * 16;
    const u32x4 r1 = mkdesc(ARG_w1, 576 * 9216);
    if (isO) {
      TRDEF
      const u32x4 ro = mkdesc(ARG_wo, 4096 * 1152);
      int hdst = ((l & 14) == 0) ? WS_H + gn * HSTR + (l & 1) * 8192 + (4 * cb + w4) * 64 + l4 * 16 : OOB;
      int va = L_VEC + l4 * 16;
      PIN(hdst); PIN(va); PIN(ta);
      flag_wait(F_VD + w4 * 4, 1u);
      dma_tiles<WO_PRE, 8>(ro, l3 * 1152 + sw16, (gn * 256 + w4 * 8) * 1152 + cb * 128, 32 * 1152, L_W + w4 * 1024, 4096);
      flag_spin(F_PUB, 1u);
      dma_tiles<8 - WO_PRE, 8>(ro, l3 * 1152 + sw16, (gn * 256 + w4 * 8 + WO_PRE * 32) * 1152 + cb * 128, 32 * 1152, L_W + w4 * 1024 + WO_PRE * 4096, 4096);
      u32x4 wf[8], of[8];
      gather_o(dws, gn, w4, l, T, []() { VMWAIT(WO_GO); flag_inc(C_G); }, [&]() {
        flag_spin(C_G, 4u);
#pragma unroll
        for (int ks = 0; ks < 8 - WO_GO; ++ks) wf[ks] = tr_frag(ta + ks * 4096);
        __builtin_amdgcn_sched_barrier(0);
      });
      f32x4 acc = {0.f, 0.f, 0.f, 0.f};
#pragma unroll
      for (int ks = 0; ks < 4; ++ks) of[ks] = lds_ld<u32x4>(va + ks * 64);
      __builtin_amdgcn_sched_barrier(0);
#pragma unroll
      for (int ks = 0; ks < 8; ++ks) {
        if (ks + 4 < 8) { if (ks + 4 >= 8 - WO_GO) wf[ks + 4] = tr_frag(ta + (ks + 4) * 4096); of[ks + 4] = lds_ld<u32x4>(va + (ks + 4) * 64); }
        acc = mfma(wf[ks], of[ks], acc);
        __builtin_amdgcn_sched_barrier(0);
      }
      __builtin_amdgcn_raw_buffer_store_b128(pack4(acc, T), rws, hdst, 0, AUX_SC1);
      flag_st(F_HS + w4 * 4, 1u);
    } else if (isM) {
      flag_spin(F_PUB, 1u);
      dma_tiles<18, PACE_W1>(r1, l3 * 9216 + sw16, w4 * 8 * 9216 + mi * 128, 32 * 9216, L_W + w4 * 1024, 4096);
      gather_h(dws, wave2, l, T, []() { VMWAIT(W1_GO); flag_inc(C_G); flag_spin(C_G, 4u); }, []() { VMWAIT(4); flag_inc(C_W); });
      if (mi == 0 && w4 == 0) __builtin_amdgcn_raw_buffer_store_b32(T32, rws, l == 0 ? WS_EPOCH : OOB, 0, AUX_SC1);
      return;
    } else {
      flag_wait(F_VD + w4 * 4, 1u);
      const int u = (rl - 27) * 4 + w4, vb = ((3 - (int)(r1.x >> 7) - 9 * xcd) & 7) + (l >> 5) * 8, row = W1_HELP_ROW + (l & 31) * 20 + u;
      pf1(r1, (vb < 9 && row < 576) ? row * 9216 + (xcd * 9 + vb) * 128 : OOB);
      VMWAIT(0);
      flag_inc(C_W);
      if (ri >= 36) return;
    }
  } else {
    ROLEDEF
    const RSRC rv = mkrsrc(vc + kvb, 53248);
    const int vo = l4 * 1024 + (l & 15) * 16;
    SELDEF
    float b1v = lo_f(__builtin_amdgcn_raw_buffer_load_b16(mkrsrc(ARG_b1, 9216), isM ? (mi * 64 + w4 * 16 + (l & 15)) * 2 : OOB, 0, AUX_NT));
    int odst = WS_O + cu * 1024 + (l >> 5) * 512 + (l & 15) * 32 + (l4 & 1) * 16;
    int pofs = L_PART + l * 16;
    PIN(odst); PIN(pofs); PIN(b1v);
    flag_wait(F_KV + w4 * 4, 1u);
    u32x4 vr[16];
#pragma unroll
    for (int n = 0; n < 16; ++n) {
      if (n >= PACE_V) VMWAIT(PACE_V - 1);
      vr[n] = __builtin_amdgcn_raw_buffer_load_b128(rv, vo + (n & 3) * 256, (((n >> 2) + s) & 3) * 16384, AUX_NT);
      __builtin_amdgcn_sched_barrier(0);
    }
    flag_wait(C_P, 4u);
    T32 = flag_ld(L_TAG);
    const u32 T = T32 & 0xffu;
    const u32x4 p0 = lds_ld<u32x4>(L_P + w4 * 256 + l3 * 32), p1 = lds_ld<u32x4>(L_P + w4 * 256 + l3 * 32 + 16);
    const u32 pp[8] = {p0.x, p0.y, p0.z, p0.w, p1.x, p1.y, p1.z, p1.w};
    const f32x4 m4 = lds_ld<f32x4>(L_MX + (l >> 5) * 16);
    float fsc = __builtin_amdgcn_exp2f((lds_ld<float>(L_MX + (l >> 5) * 16 + w4 * 4) - fmaxf(fmaxf(m4.x, m4.y), fmaxf(m4.z, m4.w))) * LOG2E);
    PIN(fsc);
    f32x4 o4 = {0.f, 0.f, 0.f, 0.f};
    u32x4 pa[SIG_V];
#pragma unroll
    for (int n = 0; n < 16 - SIG_V; ++n) o4 = mfma((n & 1) ? DIAG_HI(pp[n >> 1]) : DIAG_LO(pp[n >> 1]), vr[n], o4);
    __builtin_amdgcn_sched_barrier(0);
    flag_inc(C_ML);
    flag_st(F_VD + w4 * 4, 1u);
#pragma unroll
    for (int n = 16 - SIG_V; n < 16; ++n) pa[n - 16 + SIG_V] = (n & 1) ? DIAG_HI(pp[n >> 1]) : DIAG_LO(pp[n >> 1]);
    __builtin_amdgcn_sched_barrier(0);
#pragma unroll
    for (int n = 16 - SIG_V; n < 16; ++n) o4 = mfma(pa[n - 16 + SIG_V], vr[n], o4);
    lds_st<f32x4>(pofs + w4 * 1024, o4 * fsc);
    {
      u32 old; f32x4 t0, t1, t2, t3;
      asm volatile("s_mov_b64 exec, 1\n\tds_add_rtn_u32 %0, %5, %6\n\ts_mov_b64 exec, -1\n\tds_read_b128 %1, %7\n\tds_read_b128 %2, %7 offset:1024\n\t"
                   "ds_read_b128 %3, %7 offset:2048\n\tds_read_b128 %4, %7 offset:3072\n\ts_waitcnt lgkmcnt(0)"
                   : "=&v"(old), "=&v"(t0), "=&v"(t1), "=&v"(t2), "=&v"(t3) : "v"(C_O), "v"(1u), "v"(pofs) : "memory");
      if (__builtin_amdgcn_readfirstlane(old) == 3u) {
        __builtin_amdgcn_raw_buffer_store_b128(pack4((t0 + t1) + (t2 + t3), T), rws, odst, 0, AUX_SC1);
        flag_st(F_PUB, 1u);
      }
    }
    if (isO) {
      gather_o(dws, gn, 4 + w4, l, T, []() { flag_spin(C_G, 2u); }, []() {});
      const u32x4 r2 = mkdesc(ARG_w2, 4608 * 1152);
      const int v2 = l3 * 1152 + ((l & 7) ^ (l3 & 6)) * 16;
      flag_spin(F_HS + w4 * 4, 1u);
      dma_tiles<9, 9>(r2, v2, (pi * 32 + w4 * 8) * 1152, 128, L_K + w4 * 1024, 4096);
      VMWAIT(0);
      flag_inc(C_W2);
    } else if (isM) {
      TRDEF
      int va = L_VEC + ((l >> 2) & 3) * 384 + l4 * 16;
      int gdst = ((l & 19) == 0) ? G_OFF(mi * 2 + (w4 >> 1)) + (w4 & 1) * 64 + (l >> 5) * 8192 + (l & 15) * 4 : OOB;
      float k0 = l4 == 0 ? 1.f : 0.f, k1 = l4 == 1 ? 1.f : 0.f, k2 = l4 == 2 ? 1.f : 0.f, k3 = l4 == 3 ? 1.f : 0.f;
      PIN(va); PIN(gdst); PIN(ta); PIN(k0); PIN(k1); PIN(k2); PIN(k3);
      u32x4 wf[18];
      gather_h(dws, wave2, l, T, []() { flag_spin(C_G, 4u); }, [&]() {
        flag_spin(C_W, 4u);
#pragma unroll
        for (int ks = 0; ks < 16; ++ks) wf[ks] = tr_frag(ta + ks * 4096);
        wf[16] = tr_frag(ta + 65536); wf[17] = tr_frag(ta + 65536 + 4096);
        __builtin_amdgcn_sched_barrier(0);
      });
      asm volatile(
                   "ds_read_b128 v[104:107], %18 offset:0\n\tds_read_b128 v[108:111], %18 offset:64\n\t"
                   "ds_read_b128 v[112:115], %18 offset:128\n\tds_read_b128 v[116:119], %18 offset:192\n\t"
                   "ds_read_b128 v[120:123], %18 offset:256\n\tds_read_b128 v[124:127], %18 offset:320\n\ts_waitcnt lgkmcnt(5)\n\t"
                   "v_mfma_f32_16x16x32_bf16 v[128:131], v[104:107], v[104:107], 0\n\t"
                   "s_waitcnt lgkmcnt(4)\n\tv_mfma_f32_16x16x32_bf16 v[128:131], v[108:111], v[108:111], v[128:131]\n\t"
                   "s_waitcnt lgkmcnt(3)\n\tv_mfma_f32_16x16x32_bf16 v[128:131], v[112:115], v[112:115], v[128:131]\n\t"
                   "s_waitcnt lgkmcnt(2)\n\tv_mfma_f32_16x16x32_bf16 v[128:131], v[116:119], v[116:119], v[128:131]\n\t"
                   "s_waitcnt lgkmcnt(1)\n\tv_mfma_f32_16x16x32_bf16 v[128:131], v[120:123], v[120:123], v[128:131]\n\t"
                   "s_waitcnt lgkmcnt(0)\n\tv_mfma_f32_16x16x32_bf16 v[128:131], v[124:127], v[124:127], v[128:131]\n\t"
                   "v_mfma_f32_16x16x32_bf16 v[132:135], v[104:107], %0, 0\n\t"
                   "v_mfma_f32_16x16x32_bf16 v[132:135], v[108:111], %1, v[132:135]\n\t"
                   "v_mfma_f32_16x16x32_bf16 v[132:135], v[112:115], %2, v[132:135]\n\ts_nop 4\n\tv_readlane_b32 s70, v128, 12\n\t"
                   "v_readlane_b32 s71, v128, 0\n\t"
                   "v_mfma_f32_16x16x32_bf16 v[132:135], v[116:119], %3, v[132:135]\n\tv_readlane_b32 s72, v128, 28\n\t"
                   "v_readlane_b32 s73, v128, 20\n\t"
                   "v_mfma_f32_16x16x32_bf16 v[132:135], v[120:123], %4, v[132:135]\n\tv_readlane_b32 s74, v128, 44\n\t"
                   "v_readlane_b32 s75, v128, 40\n\tv_mov_b32 v144, s70\n\t"
                   "v_mfma_f32_16x16x32_bf16 v[132:135], v[124:127], %5, v[132:135]\n\tv_mov_b32 v145, s71\n\tv_add_f32 v144, s72, v144\n\t"
                   "v_mfma_f32_16x16x32_bf16 v[136:139], v[104:107], %6, 0\n\tv_add_f32 v145, s73, v145\n\tv_add_f32 v144, s74, v144\n\t"
                   "v_mfma_f32_16x16x32_bf16 v[136:139], v[108:111], %7, v[136:139]\n\tv_add_f32 v145, s75, v145\n\t"
                   "v_mul_f32 v144, 0x3ae38e39, v144\n\t"
                   "v_mfma_f32_16x16x32_bf16 v[136:139], v[112:115], %8, v[136:139]\n\tv_mul_f32 v145, 0x3ae38e39, v145\n\t"
                   "v_fma_f32 v145, -v144, v144, v145\n\t"
                   "v_mfma_f32_16x16x32_bf16 v[136:139], v[116:119], %9, v[136:139]\n\tv_max_f32 v145, 0, v145\n\t"
                   "v_cvt_pk_bf16_f32 v146, 0, v145\n\tv_add_f32 v146, 0x3727c5ac, v146\n\t"
                   "v_mfma_f32_16x16x32_bf16 v[136:139], v[120:123], %10, v[136:139]\n\tv_cvt_pk_bf16_f32 v146, 0, v146\n\t"
                   "v_sqrt_f32 v146, v146\n\t"
                   "v_mfma_f32_16x16x32_bf16 v[136:139], v[124:127], %11, v[136:139]\n\tv_cvt_pk_bf16_f32 v146, 0, v146\n\t"
                   "v_mul_f32 v147, v146, v146\n\t"
                   "v_mfma_f32_16x16x32_bf16 v[140:143], v[104:107], %12, 0\n\tv_rcp_f32 v147, v147\n\tv_fma_f32 v148, -v144, %26, %23\n\t"
                   "v_mfma_f32_16x16x32_bf16 v[140:143], v[108:111], %13, v[140:143]\n\tv_mul_f32 v147, v145, v147\n\t"
                   "v_cvt_pk_bf16_f32 v147, 0, v147\n\tv_add_f32 v147, 0x3727c5ac, v147\n\t"
                   "v_mfma_f32_16x16x32_bf16 v[140:143], v[112:115], %14, v[140:143]\n\tv_cvt_pk_bf16_f32 v147, 0, v147\n\t"
                   "v_sqrt_f32 v147, v147\n\tv_fma_f32 v149, -v144, %26, %24\n\t"
                   "v_mfma_f32_16x16x32_bf16 v[140:143], v[116:119], %15, v[140:143]\n\tv_cvt_pk_bf16_f32 v147, 0, v147\n\t"
                   "v_mul_f32 v147, v146, v147\n\tv_fma_f32 v150, -v144, %26, %25\n\t"
                   "v_mfma_f32_16x16x32_bf16 v[140:143], v[120:123], %16, v[140:143]\n\tv_rcp_f32 v147, v147\n\tv_mul_f32 v151, v132, v148\n\t"
                   "v_mfma_f32_16x16x32_bf16 v[140:143], v[124:127], %17, v[140:143]\n\tv_fma_f32 v151, v136, v149, v151\n\t"
                   "v_mul_f32 v150, v150, v147\n\tv_mul_f32 v151, v151, v147\n\tv_mov_b32 v153, 0xc0135761\n\ts_nop 3\n\t"
                   "v_fma_f32 v151, v140, v150, v151\n\tv_mov_b32 v152, v151\n\ts_nop 1\n\tv_permlane16_swap_b32 v151, v152\n\t"
                   "v_add_f32 v151, v151, v152\n\tv_mov_b32 v152, v151\n\ts_nop 1\n\tv_permlane32_swap_b32 v151, v152\n\t"
                   "v_add_f32 v151, v151, v152\n\tv_cvt_pk_bf16_f32 v151, 0, v151\n\tv_add_f32 v151, v151, %19\n\t"
                   "v_cvt_pk_bf16_f32 v151, 0, v151\n\tv_mul_f32 v152, v151, v151\n\tv_fmac_f32 v153, 0xbdd2d3e7, v152\n\t"
                   "v_mul_f32 v152, v153, v151\n\tv_exp_f32 v152, v152\n\ts_nop 0\n\tv_add_f32 v152, 1.0, v152\n\tv_rcp_f32 v152, v152\n\ts_nop 0\n\t"
                   "v_mul_f32 v151, v152, v151\n\tv_cvt_pk_bf16_f32 v151, 0, v151\n\tv_or_b32 v151, %22, v151\n\ts_nop 1\n\t"
                   "v_mov_b32_dpp v152, v151 quad_perm:[0,0,0,0] row_mask:0xf bank_mask:0xf\n\t"
                   "v_mov_b32_dpp v153, v151 quad_perm:[1,1,1,1] row_mask:0xf bank_mask:0xf\n\t"
                   "v_mov_b32_dpp v154, v151 quad_perm:[2,2,2,2] row_mask:0xf bank_mask:0xf\n\t"
                   "v_mov_b32_dpp v155, v151 quad_perm:[3,3,3,3] row_mask:0xf bank_mask:0xf\n\t"
                   "buffer_store_dwordx4 v[152:155], %20, %21, 0 offen sc1\n\ts_nop 1\n\t"
                   :: "v"(wf[0]), "v"(wf[1]), "v"(wf[2]), "v"(wf[3]), "v"(wf[4]), "v"(wf[5]), "v"(wf[6]), "v"(wf[7]), "v"(wf[8]), "v"(wf[9]),
                      "v"(wf[10]), "v"(wf[11]), "v"(wf[12]), "v"(wf[13]), "v"(wf[14]), "v"(wf[15]), "v"(wf[16]), "v"(wf[17]),
                      "v"(va), "v"(b1v), "v"(gdst), "s"(dws), "s"(T32 & 0xffffu), "v"(k0), "v"(k1), "v"(k2), "v"(k3)
                   : "memory", "s70", "s71", "s72", "s73", "s74", "s75",
                     "v104", "v105", "v106", "v107", "v108", "v109", "v110", "v111", "v112", "v113", "v114", "v115", "v116", "v117", "v118", "v119",
                     "v120", "v121", "v122", "v123", "v124", "v125", "v126", "v127", "v128", "v129", "v130", "v131", "v132", "v133", "v134", "v135",
                     "v136", "v137", "v138", "v139", "v140", "v141", "v142", "v143", "v144", "v145", "v146", "v147", "v148", "v149", "v150", "v151",
                     "v152", "v153", "v154", "v155");
      return;
    } else if (ri >= 36) return;
  }

  ROLEDEF
  const u32 T = T32 & 0xffu;
  if (isO) {
    const int r7 = 4 * (l4 & 1) + ((l >> 2) & 3);
    const int tb = L_K + wave2 * 4096 + (16 * (l >> 5) + r7) * 128 + (((l & 3) ^ (r7 >> 1)) * 32);
    int pdst = l < 32 ? P_OFF(pi) + l4 * 8192 + wave2 * 256 + (l & 15) * 16 : OOB;
    int gofs = G_OFF(pi) + near_half() + (l & 7) * 16;
    int gdst = L_VEC + 1024 + swap23(4 * (l & 7)) * 2, gsrc = L_VEC + 1024 + l4 * 16;
    PIN(pdst); PIN(gofs); PIN(gdst); PIN(gsrc);
    flag_spin(C_W2, 4u);
    u32x4 wf[4];
#pragma unroll
    for (int t = 0; t < 4; ++t) wf[t] = tr_frag(tb + t * 8);
    __builtin_amdgcn_sched_barrier(0);
    if (wave2 == 2) {
      int n, m;
      asm volatile("s_mov_b32 %0, %7\n\ts_mov_b64 exec, 0xff\n"
                   "1:\n\t"
                   "buffer_load_dwordx4 v[140:143], %2, %3, 0 offen sc1\n\t"
                   "s_sub_u32 %0, %0, 1\n\ts_cselect_b32 %1, 0, 0xffff\n\t"
                   "s_waitcnt vmcnt(0)\n\t"
                   "v_bitop3_b32 v144, v140, v141, %4 bitop3:0x7e\n\tv_bitop3_b32 v145, v142, v143, %4 bitop3:0x7e\n\t"
                   "v_bitop3_b32 v144, v144, %1, v145 bitop3:0xc8\n\tv_cmp_ne_u32 vcc, 0, v144\n\t"
                   "v_perm_b32 v146, v141, v140, %5\n\tv_perm_b32 v147, v143, v142, %5\n\t"
                   "s_cbranch_vccnz 1b\n\t"
                   "ds_write_b64 %6, v[146:147]\n\ts_mov_b64 exec, -1"
                   : "=&s"(n), "=&s"(m) : "v"(gofs), "s"(dws), "s"(T32), "s"(0x07060302u), "v"(gdst), "n"(SPIN_CAP) : POLL_CLOB);
    }
    BAR();
    const u32x4 gf = lds_ld<u32x4>(gsrc);
    f32x4 c[4];
#pragma unroll
    for (int t = 0; t < 4; ++t) c[t] = mfma(gf, wf[t], f32x4{0.f, 0.f, 0.f, 0.f});
    __builtin_amdgcn_raw_buffer_store_b128(pack4(f32x4{c[0].x, c[1].x, c[2].x, c[3].x}, T), rws, pdst, 0, AUX_SC1);
    return;
  }
  flag_wait(F_E, 1u);
  int po = P_OFF(wave2 * 16 + (l >> 2)) + near_half() + ri * 64 + (l & 3) * 16;
  int zdst = L_VEC + ((l & 3) + 4 * l4) * 64 + wave2 * 4;
  int zsrc = L_VEC + (4 * (l & 3) + l4) * 64 + ((l >> 2) & 3) * 16;
  int odst = ((l & 12) == 0) ? (16 * ri + 4 * (l & 3) + l4) * 2 : OOB;
  u32x4 rout = mkdesc(ARG_out, 1152);
  asm volatile("" : "+s"(rout));
  PIN(po); PIN(zdst); PIN(zsrc); PIN(odst);
  if (wave2 == 0) lds_st<u32x4>(L_VEC + l * 16, u32x4{0u, 0u, 0u, 0u});
  BAR();
  poll1_p(dws, po, T, zdst);
  if (wave2 == 0) {
    asm volatile("ds_read_b128 v[140:143], %0\n\ts_waitcnt lgkmcnt(0)\n\tv_pk_add_f32 v[140:141], v[140:141], v[142:143]\n\tv_add_f32 v140, v140, v141\n\t"
                 ROR_ADD(8) ROR_ADD(4)
                 "v_cvt_pk_bf16_f32 v140, 0, v140\n\tv_add_f32 v140, v140, %1\n\tv_cvt_pk_bf16_f32 v140, 0, v140\n\tv_add_f32 v140, v140, %2\n\t"
                 "v_cvt_pk_bf16_f32 v140, v140, v140\n\tbuffer_store_short v140, %3, %4, 0 offen"
                 :: "v"(zsrc), "v"(b2v), "v"(hv), "v"(odst), "s"(rout) : POLL_CLOB);
  }
}

}

extern "C" void kernel_launch(void* const* d_in, const int* in_sizes, int n_in, void* d_out, int out_size, void* d_ws, size_t ws_size,
                              hipStream_t stream) {
  (void)hipFuncSetAttribute((const void*)fused_decode, hipFuncAttributeMaxDynamicSharedMemorySize, LDS_BYTES);
  fused_decode<<<256, NTHR, LDS_BYTES, stream>>>((const char*)d_in[0], (const char*)d_in[1], (const char*)d_in[2], (const char*)d_in[3],
                                                 (char*)d_ws, (const char*)d_in[4], (const char*)d_in[6], (const char*)d_in[8],
                                                 (const char*)d_in[7], (const char*)d_in[9], (char*)d_out);
}
```
